```python
import torch

def _softmax(x, axis=-1):
    return torch.softmax(x, dim=axis)
def _gelu(x):
    return torch.nn.functional.gelu(x, approximate="tanh")
def _silu(x):
    return torch.nn.functional.silu(x)
def _sigmoid(x):
    return torch.sigmoid(x)
def _layernorm(x, axis=-1, eps=1e-5):
    m = x.mean(dim=axis, keepdim=True)
    v = x.var(dim=axis, keepdim=True, unbiased=False)
    return (x - m) / torch.sqrt(v + eps)
def _scatter_add(tgt, idx, src, axis=0):
    return tgt.clone().index_add_(axis, idx, src)
def _segment_sum(src, seg, n):
    out = torch.zeros((n,) + src.shape[1:], dtype=src.dtype)
    return out.index_add_(0, seg, src)
def _pad(x, pw):
    flat = tuple(v for pair in reversed(pw) for v in pair)
    return torch.nn.functional.pad(x, flat)

kernel_name = 'sequential_preLN__linear_kernel_hedgehog__dense_activation_squared_relu__prompt'

def setup_inputs():

    _w = (lambda a: (lambda t: t.to(torch.bfloat16) if t.is_floating_point() else t)(torch.as_tensor(a)))
    def _A_setup():
        B, S, D, H = 1, 2048, 512, 32
        Dh = D // H
        return {
            "x": _w((lambda *s: torch.randn(*s, dtype=torch.bfloat16) * 0.05)(B, S, D)),
            "wqkv": _w((lambda *s: torch.randn(*s, dtype=torch.bfloat16) * 0.2)(D, 3 * D)),
            "w_phi": _w((lambda *s: torch.randn(*s, dtype=torch.bfloat16) * 0.2)(H, Dh, Dh) * (Dh ** -0.5)),
            "wo": _w((lambda *s: torch.randn(*s, dtype=torch.bfloat16) * 0.05)(D, D)),
        }
    _A_in = _A_setup()

    def _B_setup():
        B, S, D, F = 1, 2048, 512, 1376
        return {
            "x": _w((lambda *s: torch.randn(*s, dtype=torch.bfloat16) * 0.05)(B, S, D)),
            "w1": _w((lambda *s: torch.randn(*s, dtype=torch.bfloat16) * 0.05)(D, F)),
            "b1": _w((lambda *s: torch.randn(*s, dtype=torch.bfloat16) * 0.4)(F)),
            "w2": _w((lambda *s: torch.randn(*s, dtype=torch.bfloat16) * 0.05)(F, D)),
            "b2": _w((lambda *s: torch.randn(*s, dtype=torch.bfloat16) * 1.6)(D)),
        }
    _B_in = _B_setup()

    return {
        "A_x": _A_in["x"],
        "A_wqkv": _A_in["wqkv"],
        "A_w_phi": _A_in["w_phi"],
        "A_wo": _A_in["wo"],
        "B_x": _B_in["x"],
        "B_w1": _B_in["w1"],
        "B_b1": _B_in["b1"],
        "B_w2": _B_in["w2"],
        "B_b2": _B_in["b2"],
    }

def reference(A_x, A_wqkv, A_w_phi, A_wo, B_x, B_w1, B_b1, B_w2, B_b2):
    def _A_ref(x, wqkv, w_phi, wo):
        B, S, D = x.shape
        H = 32; Dh = D // H
        qkv = (lambda x, *d: x.permute(*d))((x @ wqkv).reshape(B, S, 3, H, Dh), 2, 0, 3, 1, 4)
        q, k, v = qkv[0], qkv[1], qkv[2]
        pq = torch.einsum("bhsd,hdf->bhsf", q, w_phi)
        pk = torch.einsum("bhsd,hdf->bhsf", k, w_phi)
        phi_q = torch.concatenate([_softmax(pq, axis=-1), _softmax(-pq, axis=-1)], axis=-1)
        phi_k = torch.concatenate([_softmax(pk, axis=-1), _softmax(-pk, axis=-1)], axis=-1)
        kv = torch.einsum("bhsf,bhsd->bhfd", phi_k, v)
        z  = torch.einsum("bhsf->bhf", phi_k)
        num = torch.einsum("bhqf,bhfd->bhqd", phi_q, kv)
        den = torch.einsum("bhqf,bhf->bhq", phi_q, z)[..., None] + 1e-6
        o = num / den
        return (lambda x, *d: x.permute(*d))(o, 0, 2, 1, 3).reshape(B, S, D) @ wo

    def _B_ref(x, w1, b1, w2, b2):
        h = torch.einsum("bsd,df->bsf", x, w1) + b1
        zero = torch.zeros_like(h)
        r = torch.where(h > zero, h, zero)
        h = r * r
        out = torch.einsum("bsf,fd->bsd", h, w2) + b2
        return out

    x = A_x
    h = x + _A_ref(_layernorm(x, axis=-1), A_wqkv, A_w_phi, A_wo)
    h2 = h + _B_ref(_layernorm(h, axis=-1), B_w1, B_b1, B_w2, B_b2)
    return h2
```

```cpp
#include <hip/hip_runtime.h>
#include <stdint.h>

typedef unsigned short u16;
typedef short s16x4 __attribute__((ext_vector_type(4)));
typedef __bf16 bf16x2 __attribute__((ext_vector_type(2)));
typedef __bf16 bf16x8 __attribute__((ext_vector_type(8)));
typedef float f32x16 __attribute__((ext_vector_type(16)));
typedef float f32x4 __attribute__((ext_vector_type(4)));
typedef float f32x2 __attribute__((ext_vector_type(2)));
typedef unsigned u32x4 __attribute__((ext_vector_type(4)));
typedef unsigned u32x2 __attribute__((ext_vector_type(2)));
#define LAS __attribute__((address_space(3)))
#define DI __device__ __forceinline__

constexpr size_t WS_FLAGS = 0;
constexpr size_t WS_SLAB = 65536;
constexpr size_t WS_ST = (size_t)1536 << 10;
constexpr size_t WS_O = (size_t)2 << 20;
constexpr size_t WS_H = (size_t)4 << 20;
constexpr size_t WS_PQ = (size_t)12 << 20;
constexpr size_t WS_HID = (size_t)6 << 20;
constexpr unsigned XS0 = 0, XS1 = 32768, XS2 = 65536, RB = 98304, LDS_BYTES = 163840;
constexpr unsigned BK = RB, BV = RB + 32768, BQ = XS1;
constexpr unsigned SC_PHI = XS2;
constexpr unsigned SC_V = XS2 + 16384;
constexpr unsigned SC_CS = XS2 + 24576;
constexpr unsigned SC_ONES = XS2 + 25600;
constexpr unsigned SC_ZERO = XS2 + 25664;
constexpr unsigned SC_CNT = XS2 + 25728;
constexpr unsigned SC_CC = SC_CNT + 4, SC_HQ = SC_CNT + 8, SC_HS = SC_CNT + 12;
constexpr unsigned SC_ST1 = XS2 + 28672;
constexpr unsigned SC_KVB = XS2 + 26624;
constexpr unsigned SC_C = LDS_BYTES - 4096;
constexpr unsigned SPIN_CAP = 1u << 20;

DI void glds16(const void* sbase, unsigned voff, unsigned lds_dst) {
  asm volatile("s_mov_b32 m0, %2\n\ts_nop 0\n\tglobal_load_lds_dwordx4 %0, %1" :: "v"(voff), "s"(sbase), "s"(lds_dst) : "memory");
}
template <int OFF> DI u32x4 gload16_sc1(const void* sbase, unsigned voff) {
  u32x4 v; asm volatile("global_load_dwordx4 %0, %1, %2 offset:%3 sc1" : "=v"(v) : "v"(voff), "s"(sbase), "i"(OFF) : "memory"); return v;
}
DI u32x4 gload16(const void* sbase, unsigned voff) {
  u32x4 v; asm volatile("global_load_dwordx4 %0, %1, %2" : "=v"(v) : "v"(voff), "s"(sbase) : "memory"); return v;
}
template <int OFF> DI unsigned gload2(const void* sbase, unsigned voff) {
  unsigned v; asm volatile("global_load_ushort %0, %1, %2 offset:%3" : "=v"(v) : "v"(voff), "s"(sbase), "i"(OFF) : "memory"); return v;
}
template <int OFF> DI u32x2 gload8_sc1(const void* sbase, unsigned voff) {
  u32x2 v; asm volatile("global_load_dwordx2 %0, %1, %2 offset:%3 sc1" : "=v"(v) : "v"(voff), "s"(sbase), "i"(OFF) : "memory"); return v;
}
DI void gstore8_sc1(void* sbase, unsigned voff, u32x2 v) {
  asm volatile("global_store_dwordx2 %0, %1, %2 sc1" :: "v"(voff), "v"(v), "s"(sbase) : "memory");
}
template <int OFF> DI void gstore16_sc1(void* sbase, unsigned voff, u32x4 v) {
  asm volatile("global_store_dwordx4 %0, %1, %2 offset:%3 sc1\n\ts_nop 1" :: "v"(voff), "v"(v), "s"(sbase), "i"(OFF) : "memory");
}
#define WAITV(n) asm volatile("s_waitcnt vmcnt(" #n ")" ::: "memory")
#define BAR() asm volatile("s_waitcnt lgkmcnt(0)\n\ts_barrier" ::: "memory")
#define TIE(x) asm volatile("" : "+v"(x))
#define PIN2(x, y) asm volatile("" : "+v"(x), "+v"(y))
#define GAS __attribute__((address_space(1)))
#define WAITV1(n, a) asm volatile("s_waitcnt vmcnt(" #n ")" : "+v"(a))
#define WAITV2(n, a, b) asm volatile("s_waitcnt vmcnt(" #n ")" : "+v"(a), "+v"(b))
#define WAITV4(n, a, b, c, d) asm volatile("s_waitcnt vmcnt(" #n ")" : "+v"(a), "+v"(b), "+v"(c), "+v"(d))
#define SB() __builtin_amdgcn_sched_barrier(0)
#define MFMA_PAD(x) asm volatile("s_nop 7\n\ts_nop 7\n\ts_nop 3" : "+v"(x))

DI unsigned pk_bf16(float lo, float hi) { bf16x2 r; r[0] = (__bf16)lo; r[1] = (__bf16)hi; return __builtin_bit_cast(unsigned, r); }
DI float bflo(unsigned u) { return __uint_as_float(u << 16); }
DI float bfhi(unsigned u) { return __uint_as_float(u & 0xffff0000u); }
DI f32x16 mfma(u32x4 a, u32x4 b, f32x16 c) {
  return __builtin_amdgcn_mfma_f32_32x32x16_bf16(__builtin_bit_cast(bf16x8, a), __builtin_bit_cast(bf16x8, b), c, 0, 0, 0);
}
DI u32x2 trread(unsigned addr) { return __builtin_bit_cast(u32x2, __builtin_amdgcn_ds_read_tr16_b64_v4i16((LAS s16x4*)(uintptr_t)addr)); }
DI u32x4 trfrag(unsigned addr, unsigned second) { u32x2 a = trread(addr), b = trread(addr + second); return u32x4{a.x, a.y, b.x, b.y}; }
struct Frag { u32x2 lo, hi; };
DI u32x4 cat(const Frag& f) { return u32x4{f.lo.x, f.lo.y, f.hi.x, f.hi.y}; }
template <int OFF> DI void trf(Frag& f, unsigned addr) {
  asm volatile("ds_read_b64_tr_b16 %0, %2 offset:%3\n\tds_read_b64_tr_b16 %1, %2 offset:%4" : "=&v"(f.lo), "=&v"(f.hi) : "v"(addr), "i"(OFF), "i"(OFF + 256));
}
template <int OFF> DI void rd128(u32x4& d, unsigned addr) { asm volatile("ds_read_b128 %0, %1 offset:%2" : "=v"(d) : "v"(addr), "i"(OFF)); }
#define WAITL1(n, a) asm volatile("s_waitcnt lgkmcnt(" #n ")" : "+v"(a.lo), "+v"(a.hi))
#define WAITL2(n, a, b) asm volatile("s_waitcnt lgkmcnt(" #n ")" : "+v"(a.lo), "+v"(a.hi), "+v"(b.lo), "+v"(b.hi))
#define WAITL3(n, a, b, c) asm volatile("s_waitcnt lgkmcnt(" #n ")" : "+v"(a.lo), "+v"(a.hi), "+v"(b.lo), "+v"(b.hi), "+v"(c.lo), "+v"(c.hi))
#define WAITL2X(n, a, b, x) asm volatile("s_waitcnt lgkmcnt(" #n ")" : "+v"(a.lo), "+v"(a.hi), "+v"(b.lo), "+v"(b.hi), "+v"(x))
template <int I> struct IC { static constexpr int v = I; };
template <int B, int E, class F> DI void sfor(F&& f) { if constexpr (B < E) { f(IC<B>{}); sfor<B + 1, E>(f); } }
template <class T> DI T lds_ld(unsigned addr) { return *(LAS T*)(uintptr_t)addr; }
template <class T> DI void lds_st(unsigned addr, T v) { *(LAS T*)(uintptr_t)addr = v; }
DI float dot2acc(unsigned a, unsigned b, float c) {
  return __builtin_amdgcn_fdot2_f32_bf16(__builtin_bit_cast(bf16x2, a), __builtin_bit_cast(bf16x2, b), c, false);
}
DI float half_sum(float v) {
  unsigned u = __float_as_uint(v);
  auto b = __builtin_amdgcn_permlane32_swap(u, u, false, false);
  return __uint_as_float(b[0]) + __uint_as_float(b[1]);
}
DI float max3(float a, float b, float c) { float r; asm("v_max3_f32 %0, %1, %2, %3" : "=v"(r) : "v"(a), "v"(b), "v"(c)); return r; }
DI float min3(float a, float b, float c) { float r; asm("v_min3_f32 %0, %1, %2, %3" : "=v"(r) : "v"(a), "v"(b), "v"(c)); return r; }
DI void hedgehog(f32x16 p, unsigned (&o)[16]) {
  const float L2E = 1.44269504088896f;
  MFMA_PAD(p);
  float mx = max3(max3(max3(p[0], p[1], p[2]), max3(p[3], p[4], p[5]), max3(p[6], p[7], p[8])), max3(max3(p[9], p[10], p[11]), max3(p[12], p[13], p[14]), p[15]), p[0]);
  float mn = min3(min3(min3(p[0], p[1], p[2]), min3(p[3], p[4], p[5]), min3(p[6], p[7], p[8])), min3(min3(p[9], p[10], p[11]), min3(p[12], p[13], p[14]), p[15]), p[0]);
  const float cp = -mx * L2E, cn = mn * L2E;
  float ep[16], en[16];
#pragma unroll
  for (int r = 0; r < 16; ++r) { ep[r] = __builtin_amdgcn_exp2f(__builtin_fmaf(p[r], L2E, cp)); en[r] = __builtin_amdgcn_exp2f(__builtin_fmaf(p[r], -L2E, cn)); }
  float sp = (((ep[0] + ep[1]) + (ep[2] + ep[3])) + ((ep[4] + ep[5]) + (ep[6] + ep[7]))) + (((ep[8] + ep[9]) + (ep[10] + ep[11])) + ((ep[12] + ep[13]) + (ep[14] + ep[15])));
  float sn = (((en[0] + en[1]) + (en[2] + en[3])) + ((en[4] + en[5]) + (en[6] + en[7]))) + (((en[8] + en[9]) + (en[10] + en[11])) + ((en[12] + en[13]) + (en[14] + en[15])));
  const float ip = __builtin_amdgcn_rcpf(sp), in_ = __builtin_amdgcn_rcpf(sn);
#pragma unroll
  for (int r = 0; r < 8; ++r) { o[r] = pk_bf16(ep[2 * r] * ip, ep[2 * r + 1] * ip); o[8 + r] = pk_bf16(en[2 * r] * in_, en[2 * r + 1] * in_); }
}
DI u32x4 widen(unsigned a0, unsigned a1, unsigned b0, unsigned b1) {
  auto s0 = __builtin_amdgcn_permlane32_swap(a0, b0, false, false);
  auto s1 = __builtin_amdgcn_permlane32_swap(a1, b1, false, false);
  return u32x4{s0[0], s1[0], s0[1], s1[1]};
}
DI void lds_add(unsigned addr, int lane) {
  asm volatile("s_waitcnt lgkmcnt(0)" ::: "memory");
  if (lane == 0) __hip_atomic_fetch_add((LAS unsigned*)(uintptr_t)addr, 1u, __ATOMIC_RELAXED, __HIP_MEMORY_SCOPE_WORKGROUP);
}
DI void lds_wait(unsigned addr, unsigned want) {
  while ((unsigned)__builtin_amdgcn_readfirstlane(__hip_atomic_load((LAS unsigned*)(uintptr_t)addr, __ATOMIC_RELAXED, __HIP_MEMORY_SCOPE_WORKGROUP)) < want) __builtin_amdgcn_s_sleep(0);
  asm volatile("" ::: "memory");
}
#define CBAR(n) do { lds_add(SC_CC, lane); lds_wait(SC_CC, 4u * (n)); } while (0)
DI void flag_add(unsigned* flag) { __hip_atomic_fetch_add((GAS unsigned*)flag, 1u, __ATOMIC_RELAXED, __HIP_MEMORY_SCOPE_AGENT); }
DI void poll_flag(unsigned* flag, unsigned want, unsigned* tmo) {
  for (unsigned spins = 0;; ++spins) {
    unsigned v = __builtin_amdgcn_readfirstlane(__hip_atomic_load((GAS unsigned*)flag, __ATOMIC_RELAXED, __HIP_MEMORY_SCOPE_AGENT));
    if (v >= want) break;
    __builtin_amdgcn_s_sleep(1);
    if (spins > SPIN_CAP) { __hip_atomic_store((GAS unsigned*)tmo, 1u, __ATOMIC_RELAXED, __HIP_MEMORY_SCOPE_AGENT); break; }
  }
}

#define PROLOGUE \
  const int tid = threadIdx.x, lane = tid & 63; \
  const int w = __builtin_amdgcn_readfirstlane(tid >> 6); \
  const int bx = blockIdx.x, pi = ((bx >> 7) << 3) | (bx & 7), j = (bx >> 3) & 15;     \
  const int pa = ((bx & 6) << 1) | (bx >> 6), ja = ((bx & 1) << 3) | ((bx >> 3) & 7);      \
  const int r32 = lane & 31, h5 = lane >> 5, blk = (lane >> 4) & 1; \
  unsigned* flags = (unsigned*)(ws + WS_FLAGS); \
  const char* xb = (const char*)x; \
  const unsigned ONES2 = 0x3f803f80u; \
  const unsigned pv_row = (unsigned)(lane >> 2), pv_col = (unsigned)((lane & 3) * 16); \
  const unsigned tr = (unsigned)((8 * h5 + ((lane >> 2) & 3)) * 64 + blk * 32 + 8 * (lane & 3)); \
  const int m = w & 3, role = w >> 2, hw = w - 4;

__global__ __launch_bounds__(512, 2) void attn_front(const u16* __restrict__ x, const u16* __restrict__ wqkv, const u16* __restrict__ wphi, const u16* __restrict__ wo,
                                                     const u16* __restrict__ w1, const u16* __restrict__ w2, char* __restrict__ ws) {
  asm volatile("" : "+s"(x), "+s"(wqkv), "+s"(wphi), "+s"(wo), "+s"(w1), "+s"(w2), "+s"(ws));
  PROLOGUE
  (void)pi; (void)j;
  const int rot = (pa + ja) & 3;
  const f32x16 ZERO16 = {};
  const int hrho = (r32 >> 2) & 1, frho = (r32 & 3) + 4 * (r32 >> 3);
  unsigned wpe[8];
  {
    const char* wp = (const char*)wphi + ja * 1024;
    const unsigned po = (unsigned)(hrho * 512 + 4 * h5 * 32 + frho * 2);
    wpe[0] = gload2<0>(wp, po); wpe[1] = gload2<32>(wp, po); wpe[2] = gload2<64>(wp, po); wpe[3] = gload2<96>(wp, po);
    wpe[4] = gload2<256>(wp, po); wpe[5] = gload2<288>(wp, po); wpe[6] = gload2<320>(wp, po); wpe[7] = gload2<352>(wp, po);
  }
  auto finalize = [&](const f32x16& acc, unsigned cs_addr, float rstd, float nmr, unsigned (&pk)[8]) {
#pragma unroll
    for (int a = 0; a < 4; ++a) {
      const f32x4 c4 = lds_ld<f32x4>(cs_addr + (unsigned)(8 * a + 4 * h5) * 4);
      pk[2 * a] = pk_bf16(acc[4 * a] * rstd + nmr * c4[0], acc[4 * a + 1] * rstd + nmr * c4[1]);
      pk[2 * a + 1] = pk_bf16(acc[4 * a + 2] * rstd + nmr * c4[2], acc[4 * a + 3] * rstd + nmr * c4[3]);
    }
  };
  auto project = [&](const unsigned (&pk)[8], unsigned (&ph)[16]) {
    u32x4 wa0, wa1;
#pragma unroll
    for (int e = 0; e < 4; ++e) { const unsigned v = wpe[2 * e] | (wpe[2 * e + 1] << 16); wa0[e] = hrho ? 0u : v; wa1[e] = hrho ? v : 0u; }
    f32x16 p = mfma(wa0, u32x4{pk[0], pk[1], pk[2], pk[3]}, ZERO16);
    p = mfma(wa1, u32x4{pk[4], pk[5], pk[6], pk[7]}, p);
    hedgehog(p, ph);
  };
  const unsigned xfer = XS0 + (unsigned)(m * 8192 + lane * 16);
  if (role == 0) {
      WAITV4(0, wpe[0], wpe[1], wpe[2], wpe[3]); TIE(wpe[4]); TIE(wpe[5]); TIE(wpe[6]); TIE(wpe[7]);
      u32x4 xf[32];
      f32x16 ak = {}, av = {};
      float ssum0 = 0.f, ssq0 = 0.f, ssum1 = 0.f, ssq1 = 0.f;
      unsigned xa[4];
      const unsigned xa0 = (unsigned)((32 * m + r32) * 256 + ((h5 ^ (r32 & 15)) * 16));
#pragma unroll
      for (int e = 0; e < 4; ++e) xa[e] = xa0 ^ (unsigned)(32 * e);
      unsigned xb4[4];
#pragma unroll
      for (int e = 0; e < 4; ++e) xb4[e] = xa[e] ^ 128u;
      unsigned trb = BK + tr;
      Frag bk[3], bv[3];
      auto rd = [&](auto KS) {
        constexpr int ks = decltype(KS)::v, c = ks >> 3;
        constexpr int slot = c == 1 ? XS1 : (c == 2 ? XS2 : XS0);
        if constexpr (slot < 65536) rd128<slot>(xf[ks], (ks & 4) ? xb4[ks & 3] : xa[ks & 3]);
        else rd128<slot - 32768>(xf[ks], ((ks & 4) ? xb4[ks & 3] : xa[ks & 3]) + 32768u);
        trf<ks * 1024>(bk[ks % 3], trb); trf<32768 + ks * 1024>(bv[ks % 3], trb);
      };
      BAR();
      rd(IC<0>{}); rd(IC<1>{});
      sfor<0, 32>([&](auto KS) {
        constexpr int ks = decltype(KS)::v;
        if constexpr ((ks & 7) == 6 && ks < 30) BAR();
        if constexpr (ks < 30) { rd(IC<ks + 2>{}); WAITL2X(10, bk[ks % 3], bv[ks % 3], xf[ks]); }
        else if constexpr (ks == 30) WAITL2X(5, bk[ks % 3], bv[ks % 3], xf[ks]);
        else WAITL2X(0, bk[ks % 3], bv[ks % 3], xf[ks]);
        const u32x4 xs = xf[ks];
        ak = mfma(cat(bk[ks % 3]), xs, ak); av = mfma(cat(bv[ks % 3]), xs, av);
        PIN2(ak, av);
        ssum0 = dot2acc(xs[0], ONES2, ssum0); ssq0 = dot2acc(xs[0], xs[0], ssq0); ssum1 = dot2acc(xs[1], ONES2, ssum1); ssq1 = dot2acc(xs[1], xs[1], ssq1);
        ssum0 = dot2acc(xs[2], ONES2, ssum0); ssq0 = dot2acc(xs[2], xs[2], ssq0); ssum1 = dot2acc(xs[3], ONES2, ssum1); ssq1 = dot2acc(xs[3], xs[3], ssq1);
        SB();
      });
      const float mean = half_sum(ssum0 + ssum1) * (1.f / 512.f);
      const float var = fmaxf(half_sum(ssq0 + ssq1) * (1.f / 512.f) - mean * mean, 0.f);
      const float rstd = __builtin_amdgcn_rsqf(var + 1e-5f), nmr = -mean * rstd;
      BAR();
#pragma unroll
      for (int a = 0; a < 4; ++a) {
        lds_st<f32x4>(xfer + a * 1024, f32x4{ak[4 * a], ak[4 * a + 1], ak[4 * a + 2], ak[4 * a + 3]});
        lds_st<f32x4>(xfer + 4096 + a * 1024, f32x4{av[4 * a], av[4 * a + 1], av[4 * a + 2], av[4 * a + 3]});
      }
      if (h5 == 0) lds_st<f32x2>(SC_ST1 + (unsigned)(32 * m + r32) * 8, f32x2{rstd, nmr});
      BAR();
      {
        f32x16 aq = {};
        float c0 = 0.f, c1 = 0.f;
        unsigned trq = BQ + tr;
        Frag bq[5];
        trf<0>(bq[0], trq); trf<1024>(bq[1], trq); trf<2048>(bq[2], trq); trf<3072>(bq[3], trq);
        sfor<0, 32>([&](auto KS) {
          constexpr int ks = decltype(KS)::v;
          if constexpr (ks < 28) { trf<(ks + 4) * 1024>(bq[(ks + 4) % 5], trq); WAITL1(8, bq[ks % 5]); }
          else if constexpr (ks == 28) WAITL1(6, bq[ks % 5]); else if constexpr (ks == 29) WAITL1(4, bq[ks % 5]);
          else if constexpr (ks == 30) WAITL1(2, bq[ks % 5]); else WAITL1(0, bq[ks % 5]);
          const u32x4 qs = cat(bq[ks % 5]);
          aq = mfma(qs, xf[ks], aq);
          TIE(aq);
          c0 = dot2acc(qs[0], ONES2, c0); c1 = dot2acc(qs[1], ONES2, c1); c0 = dot2acc(qs[2], ONES2, c0); c1 = dot2acc(qs[3], ONES2, c1);
          SB();
        });
        const float csq = half_sum(c0 + c1);
        const unsigned csa = SC_KVB + (unsigned)m * 128;
        if (h5 == 0) lds_st<float>(csa + (unsigned)r32 * 4, csq);
        BAR();
        unsigned qk[8], phiq[16];
        finalize(aq, csa, rstd, nmr, qk);
        project(qk, phiq);
        GAS u32x4* pq = (GAS u32x4*)(ws + WS_PQ + (size_t)(bx * 4 + w) * 4096) + lane;
#pragma unroll
        for (int e = 0; e < 4; ++e) pq[64 * e] = u32x4{phiq[4 * e], phiq[4 * e + 1], phiq[4 * e + 2], phiq[4 * e + 3]};
      }
  } else {
    unsigned xv[4];
#pragma unroll
    for (int n = 0; n < 4; ++n) xv[n] = (unsigned)((lane >> 4) * 1024 + ((((lane & 15) ^ (lane >> 4)) * 16) ^ (64 * n)));
    const unsigned wv_ = pv_row * 3072 + pv_col;
    const char* xrow = xb + (size_t)(pa * 128 + 32 * hw) * 1024;
    const char* wrow = (const char*)wqkv + (size_t)(32 * hw) * 3072 + ja * 64;
    auto issue_x = [&](int cc, unsigned slot) {
      const int c = (cc + rot) & 3;
#pragma unroll
      for (int n = 0; n < 8; ++n) glds16(xrow + n * 4096 + c * 256, xv[n & 3], slot + (unsigned)(32 * hw + 4 * n) * 256);
    };
    auto issue_w = [&](int cc, int seg, unsigned img) {
      const int c = (cc + rot) & 3;
#pragma unroll
      for (int n = 0; n < 2; ++n) glds16(wrow + (size_t)(c * 128 + 16 * n) * 3072 + seg * 1024, wv_, img + (unsigned)(128 * cc + 32 * hw + 16 * n) * 64);
    };
    issue_x(0, XS0); issue_w(0, 1, BK); issue_w(0, 2, BV);
    issue_x(1, XS1); issue_w(1, 1, BK); issue_w(1, 2, BV);
    issue_x(2, XS2); issue_w(2, 1, BK); issue_w(2, 2, BV); issue_w(3, 1, BK); issue_w(3, 2, BV);
    float c0 = 0.f, c1 = 0.f;
    unsigned trc = (hw == 0 ? BK : (hw == 1 ? BV : BQ)) + tr;
    auto colsum8 = [&](unsigned base) {
      u32x4 cf[8];
#pragma unroll
      for (int s = 0; s < 8; ++s) cf[s] = trfrag(base + s * 1024, 256);
#pragma unroll
      for (int s = 0; s < 8; ++s) { c0 = dot2acc(cf[s][0], ONES2, c0); c1 = dot2acc(cf[s][1], ONES2, c1); c0 = dot2acc(cf[s][2], ONES2, c0); c1 = dot2acc(cf[s][3], ONES2, c1); }
      SB();
    };
    WAITV4(28, wpe[0], wpe[1], wpe[2], wpe[3]); TIE(wpe[4]); TIE(wpe[5]); TIE(wpe[6]); TIE(wpe[7]);
    BAR();
    if (hw < 2) colsum8(trc);
    WAITV(16); BAR();
    issue_x(3, XS0);
    if (hw < 2) colsum8(trc + 8192);
    WAITV(12); BAR();
#pragma unroll
    for (int c = 0; c < 4; ++c) issue_w(c, 0, BQ);
    if (hw < 2) colsum8(trc + 16384);
    WAITV(8); BAR();
    if (hw < 2) {
      colsum8(trc + 24576);
      const float c = half_sum(c0 + c1);
      if (h5 == 0) lds_st<float>(SC_CS + (unsigned)(hw * 32 + r32) * 4, c);
    }
    if (w == 7) {
      if (lane < 16) lds_st<unsigned>(SC_ONES + (unsigned)lane * 4, ONES2);
      if (bx < 65 && lane == 0) ((GAS unsigned*)flags)[64 * bx] = 0u;
    }
    BAR();
    u32x4 wt;
    unsigned wtc;
    {
      unsigned klo, khi;
      asm volatile("s_getpc_b64 s[90:91]\n\ts_add_u32 %0, s90, _Z10block_tailPKtS0_S0_S0_S0_S0_PtPc@rel32@lo+4\n\ts_addc_u32 %1, s91, _Z10block_tailPKtS0_S0_S0_S0_S0_PtPc@rel32@hi+12" : "=s"(klo), "=s"(khi) :: "s90", "s91", "scc");
      const char* kc = (const char*)(((unsigned long long)khi << 32) | klo);
      asm volatile("global_load_dword %0, %1, %2" : "=v"(wtc) : "v"((unsigned)((hw * 64 + lane) < 168 ? (hw * 64 + lane) : lane) * 128u), "s"(kc) : "memory");
      const int xr = bx >> 3;
      const char* wsel = hw < 2 ? (const char*)w1 : (const char*)w2;
      const unsigned base = (unsigned)(xr * 344 + (hw & 1) * 172);
      asm volatile("global_load_dword %0, %1, %2" : "=v"(wt.x) : "v"((base + lane) * 128u), "s"(wsel) : "memory");
      asm volatile("global_load_dword %0, %1, %2" : "=v"(wt.y) : "v"((base + 64 + lane) * 128u), "s"(wsel) : "memory");
      asm volatile("global_load_dword %0, %1, %2" : "=v"(wt.z) : "v"((base + 128 + (lane < 44 ? lane : 0)) * 128u), "s"(wsel) : "memory");
      asm volatile("global_load_dword %0, %1, %2" : "=v"(wt.w) : "v"((unsigned)(xr * 128 + hw * 32 + r32) * 128u), "s"((const char*)wo) : "memory");
    }
    WAITV(5);
    BAR();
    {
      f32x16 ak, av;
#pragma unroll
      for (int a = 0; a < 4; ++a) {
        const f32x4 u = lds_ld<f32x4>(xfer + a * 1024), v = lds_ld<f32x4>(xfer + 4096 + a * 1024);
#pragma unroll
        for (int r = 0; r < 4; ++r) { ak[4 * a + r] = u[r]; av[4 * a + r] = v[r]; }
      }
      const f32x2 st = lds_ld<f32x2>(SC_ST1 + (unsigned)(32 * m + r32) * 8);
      unsigned pk[8], ph[16];
      finalize(av, SC_CS + 128, st[0], st[1], pk);
      const unsigned va = SC_V + (unsigned)((32 * m + r32) * 64 + 8 * h5);
#pragma unroll
      for (int a = 0; a < 4; ++a) lds_st<u32x2>(va + a * 16, u32x2{pk[2 * a], pk[2 * a + 1]});
      finalize(ak, SC_CS, st[0], st[1], pk);
      project(pk, ph);
      const unsigned pa_ = SC_PHI + (unsigned)(h5 * 8192 + (32 * m + r32) * 64);
#pragma unroll
      for (int e = 0; e < 4; ++e) lds_st<u32x4>(pa_ + e * 16, u32x4{ph[4 * e], ph[4 * e + 1], ph[4 * e + 2], ph[4 * e + 3]});
    }
    BAR();
    if (hw < 2) {
      unsigned ta = SC_PHI + (unsigned)hw * 8192 + tr;
      unsigned tb = blk ? SC_ONES + 8 * (lane & 3) : SC_V + (unsigned)((8 * h5 + ((lane >> 2) & 3)) * 64 + hw * 32 + 8 * (lane & 3));
      const unsigned tbs = blk ? 0u : 1024u, tbh = blk ? 0u : 256u;
      f32x16 kv = {};
      u32x4 fa[8], fb[8];
#pragma unroll
      for (int s = 0; s < 8; ++s) { fa[s] = trfrag(ta + s * 1024, 256); fb[s] = trfrag(tb + s * tbs, tbh); }
#pragma unroll
      for (int s = 0; s < 8; ++s) kv = mfma(fa[s], fb[s], kv);
      if (r32 < 20) {
        GAS f32x4* sb = (GAS f32x4*)(ws + WS_SLAB + (size_t)(ja * 16 + pa) * 5120 + hw * 2560 + r32 * 32 + h5 * 16);
#pragma unroll
        for (int a = 0; a < 4; ++a) sb[40 * a] = f32x4{kv[4 * a], kv[4 * a + 1], kv[4 * a + 2], kv[4 * a + 3]};
      }
    }
    WAITV2(0, wt, wtc);
  }
}

__global__ __launch_bounds__(512, 2) void block_tail(const u16* __restrict__ x, const u16* __restrict__ wo, const u16* __restrict__ w1, const u16* __restrict__ b1,
                                                     const u16* __restrict__ w2, const u16* __restrict__ b2, u16* __restrict__ out, char* __restrict__ ws) {
  asm volatile("" : "+s"(x), "+s"(wo), "+s"(w1), "+s"(b1), "+s"(w2), "+s"(b2), "+s"(out), "+s"(ws));
  PROLOGUE
  unsigned* tmo = flags + 64 * 64;
  const char* w1b = (const char*)w1 + (size_t)(j < 15 ? j : 14) * 192;
  const char* w1c = (const char*)w1 + (size_t)(j < 14 ? j : 13) * 192;
  const unsigned w1v = pv_row * 2752 + pv_col, wsq = pv_row * 1024 + pv_col;
  const int rbase = pi * 128 + (j >> 3) * 64, cbase = (j & 7) * 64;
  const int rb = j & 3;
  const int nimg = j < 14 ? 3 : (j == 14 ? 1 : 0);
  if (role == 0) {
    const int mh = w & 1, kh = w >> 1;
    u32x4 pq4[4];
    {
      const char* pqb = ws + WS_PQ + (size_t)(bx * 4 + w) * 4096;
#pragma unroll
      for (int e = 0; e < 4; ++e) pq4[e] = gload16(pqb + e * 1024, lane * 16);
    }
    {
      const char* sb = ws + WS_SLAB + (size_t)ja * 16 * 5120;
      const unsigned eo = (unsigned)((tid >> 5) * 640 + (tid & 31) * 16), zi = (unsigned)(tid >> 4);
      u32x4 sl[16];
#pragma unroll
      for (int p = 0; p < 16; ++p) sl[p] = gload16_sc1<0>(sb + p * 5120, eo);
      u32x4 sx = gload16_sc1<0>(sb, (unsigned)(lane & 15) * 5120 + (zi >> 1) * 640 + 512 + (zi & 1) * 16);
      BAR();
      WAITV(0);
      f32x4 a = {}, b = {};
#pragma unroll
      for (int p = 0; p < 16; p += 2) { TIE(sl[p]); TIE(sl[p + 1]); a += __builtin_bit_cast(f32x4, sl[p]); b += __builtin_bit_cast(f32x4, sl[p + 1]); }
      a += b;
      lds_st<u32x2>(SC_KVB + (unsigned)((tid >> 7) * 1088 + ((tid >> 1) & 15) * 64 + ((tid >> 5) & 3) * 16 + (tid & 1) * 8), u32x2{pk_bf16(a[0], a[1]), pk_bf16(a[2], a[3])});
      TIE(sx);
      f32x4 c = __builtin_bit_cast(f32x4, sx);
#pragma unroll
      for (int e = 0; e < 4; ++e) {
        float v = c[e];
        v += __builtin_bit_cast(float, __builtin_amdgcn_update_dpp(0, __builtin_bit_cast(int, v), 0x128, 0xf, 0xf, false));
        v += __builtin_bit_cast(float, __builtin_amdgcn_update_dpp(0, __builtin_bit_cast(int, v), 0x124, 0xf, 0xf, false));
        v += __builtin_bit_cast(float, __builtin_amdgcn_update_dpp(0, __builtin_bit_cast(int, v), 0x122, 0xf, 0xf, false));
        v += __builtin_bit_cast(float, __builtin_amdgcn_update_dpp(0, __builtin_bit_cast(int, v), 0x121, 0xf, 0xf, false));
        c[e] = v;
      }
      if ((lane & 15) == 0) lds_st<u32x2>(SC_KVB + (zi >> 3) * 1088 + 1024 + (zi & 7) * 8, u32x2{pk_bf16(c[0], c[1]), pk_bf16(c[2], c[3])});
    }
    CBAR(1);
    {
      const bool act = blk == h5;
      const unsigned ka = act ? SC_KVB + (unsigned)(h5 * 1088 + (r32 & 15) * 64) : SC_ZERO;
      const unsigned za = act ? SC_KVB + (unsigned)(h5 * 1088 + 1024) : SC_ZERO;
      f32x16 num = {}, den = {};
#pragma unroll
      for (int s = 0; s < 4; ++s) {
        TIE(pq4[s]);
        const u32x4 pb = pq4[s];
        num = mfma(lds_ld<u32x4>(ka + s * 16), pb, num);
        den = mfma(lds_ld<u32x4>(za + s * 16), pb, den);
      }
      const float i0 = 1.0f / (den[0] + 1e-6f), i1 = 1.0f / (den[8] + 1e-6f);
      unsigned ok[8];
#pragma unroll
      for (int e = 0; e < 4; ++e) { ok[e] = pk_bf16(num[2 * e] * i0, num[2 * e + 1] * i0); ok[4 + e] = pk_bf16(num[8 + 2 * e] * i1, num[9 + 2 * e] * i1); }
      char* ob = ws + WS_O + (size_t)((pa * 4 + m) * 32 + 2 * ja) * 1024;
      gstore16_sc1<0>(ob, lane * 16, widen(ok[0], ok[1], ok[2], ok[3]));
      gstore16_sc1<1024>(ob, lane * 16, widen(ok[4], ok[5], ok[6], ok[7]));
      WAITV(0);
    }
    CBAR(2);
    if (tid == 0) flag_add(flags + 64 * (16 + pa));
    unsigned hk[8];
    {
      f32x16 acc[2];
      {
        const GAS u32x2* xp = (const GAS u32x2*)(xb + (size_t)(rbase + 32 * mh + r32) * 1024 + (cbase + 32 * kh + 4 * h5) * 2);
        f32x16 xr;
#pragma unroll
        for (int a = 0; a < 4; ++a) { const u32x2 v = xp[2 * a]; xr[4 * a] = bflo(v.x); xr[4 * a + 1] = bfhi(v.x); xr[4 * a + 2] = bflo(v.y); xr[4 * a + 3] = bfhi(v.y); }
#pragma unroll
        for (int e = 0; e < 16; ++e) { acc[0][e] = kh ? 0.f : xr[e]; acc[1][e] = kh ? xr[e] : 0.f; }
        TIE(acc[0]); TIE(acc[1]);
      }
      if (w == 0) poll_flag(flags + 64 * (16 + pi), 16u, tmo);
      CBAR(3); lds_wait(SC_HQ, 4u);
      u32x4 af[16];
      Frag bf[4][2];
      unsigned tb[4];
#pragma unroll
      for (int q = 0; q < 4; ++q) tb[q] = RB + (unsigned)(kh * 32768 + ((q + rb) & 3) * 8192) + tr;
      auto ldb = [&](auto KS) { constexpr int ks = decltype(KS)::v; trf<(ks & 3) * 2048>(bf[ks & 3][0], tb[ks >> 2]); trf<(ks & 3) * 2048 + 1024>(bf[ks & 3][1], tb[ks >> 2]); };
      const char* ob = ws + WS_O + (size_t)(((rbase >> 5) + mh) * 32 + 16 * kh) * 1024;
#pragma unroll
      for (int q = 0; q < 4; ++q) {
        const char* oq = ob + ((q + rb) & 3) * 4096;
        af[4 * q + 0] = gload16_sc1<0>(oq, lane * 16); af[4 * q + 1] = gload16_sc1<1024>(oq, lane * 16);
        af[4 * q + 2] = gload16_sc1<2048>(oq, lane * 16); af[4 * q + 3] = gload16_sc1<3072>(oq, lane * 16);
      }
      ldb(IC<0>{}); ldb(IC<1>{}); ldb(IC<2>{}); ldb(IC<3>{});
      CBAR(4);
      sfor<0, 16>([&](auto KS) {
        constexpr int ks = decltype(KS)::v;
        if constexpr (ks >= 1 && ks < 13) WAITL2(8, bf[ks & 3][0], bf[ks & 3][1]);
        else if constexpr (ks == 13) WAITL2(8, bf[ks & 3][0], bf[ks & 3][1]);
        else if constexpr (ks == 14) WAITL2(4, bf[ks & 3][0], bf[ks & 3][1]);
        else WAITL2(0, bf[ks & 3][0], bf[ks & 3][1]);
        asm volatile("s_waitcnt vmcnt(%1)" : "+v"(af[ks]) : "i"(15 - ks));
        acc[0] = mfma(cat(bf[ks & 3][0]), af[ks], acc[0]); acc[1] = mfma(cat(bf[ks & 3][1]), af[ks], acc[1]);
        PIN2(acc[0], acc[1]);
        if constexpr (ks < 12) ldb(IC<ks + 4>{});
        SB();
      });
#pragma unroll
      for (int a = 0; a < 4; ++a) {
        f32x4 v;
#pragma unroll
        for (int r = 0; r < 4; ++r) v[r] = kh ? acc[0][4 * a + r] : acc[1][4 * a + r];
        lds_st<f32x4>(RB + (unsigned)(kh * 32768 + rb * 8192 + mh * 4096 + a * 1024 + lane * 16), v);
      }
      BAR();
      float hv[16];
#pragma unroll
      for (int a = 0; a < 4; ++a) {
        const f32x4 v = lds_ld<f32x4>(RB + (unsigned)((kh ^ 1) * 32768 + rb * 8192 + mh * 4096 + a * 1024 + lane * 16));
#pragma unroll
        for (int r = 0; r < 4; ++r) hv[4 * a + r] = (kh ? acc[1][4 * a + r] : acc[0][4 * a + r]) + v[r];
      }
#pragma unroll
      for (int e = 0; e < 8; ++e) hk[e] = pk_bf16(hv[2 * e], hv[2 * e + 1]);
      char* hb = ws + WS_H + (size_t)(((rbase >> 5) + mh) * 32 + (cbase >> 4) + 2 * kh) * 1024;
      gstore16_sc1<0>(hb, lane * 16, widen(hk[0], hk[1], hk[2], hk[3]));
      gstore16_sc1<1024>(hb, lane * 16, widen(hk[4], hk[5], hk[6], hk[7]));
      float s0 = 0.f, s1 = 0.f, q0 = 0.f, q1 = 0.f;
#pragma unroll
      for (int e = 0; e < 8; ++e) { s0 += hv[2 * e]; s1 += hv[2 * e + 1]; q0 = __builtin_fmaf(hv[2 * e], hv[2 * e], q0); q1 = __builtin_fmaf(hv[2 * e + 1], hv[2 * e + 1], q1); }
      const float ps = half_sum(s0 + s1), pq = half_sum(q0 + q1);
      if (h5 == 0) gstore8_sc1(ws + WS_ST + (size_t)(((rbase >> 5) + mh) * 16 + (j & 7) * 2 + kh) * 256, (unsigned)r32 * 8, u32x2{__float_as_uint(ps), __float_as_uint(pq)});
      WAITV(0);
      BAR();
      if (tid == 0) flag_add(flags + 64 * (32 + pi));
    }
    if (w == 0) poll_flag(flags + 64 * (32 + pi), 16u, tmo);
    BAR();
    if (nimg) {
      u32x4 af[16];
      u32x2 sp[8];
      const char* hb = ws + WS_H + (size_t)((pi * 4 + m) * 32) * 1024;
      auto lda = [&](auto KS) { constexpr int ks = decltype(KS)::v; af[ks & 15] = gload16_sc1<(ks & 3) * 1024>(hb + (((ks >> 3) + rb) & 3) * 8192 + ((ks >> 2) & 1) * 4096, lane * 16); };
      sfor<0, 16>(lda);
      f32x16 acc[3] = {};
      unsigned tc0[4], tc2[4];
#pragma unroll
      for (int q = 0; q < 4; ++q) { tc0[q] = tr + (unsigned)(((q + rb) & 3) * 8192); tc2[q] = tc0[q] + 65536; }
      Frag bf[3][3];
      auto ldb = [&](auto KS) { constexpr int ks = decltype(KS)::v; trf<(ks & 7) * 1024>(bf[ks % 3][0], tc0[ks >> 3]); trf<32768 + (ks & 7) * 1024>(bf[ks % 3][1], tc0[ks >> 3]); trf<(ks & 7) * 1024>(bf[ks % 3][2], tc2[ks >> 3]); };
      ldb(IC<0>{}); ldb(IC<1>{});
      sfor<0, 32>([&](auto KS) {
        constexpr int ks = decltype(KS)::v;
        if constexpr (ks < 30) { ldb(IC<ks + 2>{}); WAITL3(12, bf[ks % 3][0], bf[ks % 3][1], bf[ks % 3][2]); }
        else if constexpr (ks == 30) WAITL3(6, bf[ks % 3][0], bf[ks % 3][1], bf[ks % 3][2]);
        else WAITL3(0, bf[ks % 3][0], bf[ks % 3][1], bf[ks % 3][2]);
        asm volatile("s_waitcnt vmcnt(%1)" : "+v"(af[ks & 15]) : "i"(ks <= 16 ? 15 : 31 - ks + 8));
#pragma unroll
        for (int t = 0; t < 3; ++t) acc[t] = mfma(cat(bf[ks % 3][t]), af[ks & 15], acc[t]);
        PIN2(acc[0], acc[1]); TIE(acc[2]);
        if constexpr (ks < 16) lda(IC<ks + 16>{});
        if constexpr (ks == 16) {
          const char* stb = ws + WS_ST + (size_t)(pi * 4 + m) * 4096;
          const unsigned so = (unsigned)(h5 * 2048 + r32 * 8);
          sp[0] = gload8_sc1<0>(stb, so); sp[1] = gload8_sc1<256>(stb, so); sp[2] = gload8_sc1<512>(stb, so); sp[3] = gload8_sc1<768>(stb, so);
          sp[4] = gload8_sc1<1024>(stb, so); sp[5] = gload8_sc1<1280>(stb, so); sp[6] = gload8_sc1<1536>(stb, so); sp[7] = gload8_sc1<1792>(stb, so);
        }
        SB();
      });
      WAITV4(0, sp[0], sp[1], sp[2], sp[3]); TIE(sp[4]); TIE(sp[5]); TIE(sp[6]); TIE(sp[7]);
      float ssum = 0.f, ssq = 0.f;
#pragma unroll
      for (int e = 0; e < 8; ++e) { ssum += __uint_as_float(sp[e].x); ssq += __uint_as_float(sp[e].y); }
      const float mean = half_sum(ssum) * (1.f / 512.f);
      const float var = fmaxf(half_sum(ssq) * (1.f / 512.f) - mean * mean, 0.f);
      const float rstd = __builtin_amdgcn_rsqf(var + 1e-5f), nmr = -mean * rstd;
      char* hidb = ws + WS_HID + (size_t)((pi * 4 + m) * 86 + 6 * j) * 1024;
      f32x4 c4[3][4], bb[3][4];
#pragma unroll
      for (int t = 0; t < 3; ++t)
#pragma unroll
        for (int a = 0; a < 4; ++a) { c4[t][a] = lds_ld<f32x4>(SC_C + (unsigned)(t * 32 + 8 * a + 4 * h5) * 4); bb[t][a] = lds_ld<f32x4>(SC_C + 512 + (unsigned)(t * 32 + 8 * a + 4 * h5) * 4); }
#pragma unroll
      for (int t = 0; t < 3; ++t) {
        unsigned gk[8];
#pragma unroll
        for (int a = 0; a < 4; ++a) {
          float v[4];
#pragma unroll
          for (int r = 0; r < 4; ++r) { const float u = acc[t][4 * a + r] * rstd + (nmr * c4[t][a][r] + bb[t][a][r]); v[r] = u * fmaxf(u, 0.f); }
          gk[2 * a] = pk_bf16(v[0], v[1]); gk[2 * a + 1] = pk_bf16(v[2], v[3]);
        }
        const u32x4 h0 = widen(gk[0], gk[1], gk[2], gk[3]), h1 = widen(gk[4], gk[5], gk[6], gk[7]);
        if (t < nimg) { gstore16_sc1<0>(hidb + t * 2048, lane * 16, h0); gstore16_sc1<1024>(hidb + t * 2048, lane * 16, h1); }
      }
      WAITV(0);
    }
    BAR();
    if (tid == 0 && nimg) flag_add(flags + 64 * (48 + pi));
    {
      f32x16 acc[2];
      {
        const GAS u32x2* bp = (const GAS u32x2*)((const char*)b2 + (cbase + 32 * kh + 4 * h5) * 2);
        f32x16 xr;
#pragma unroll
        for (int a = 0; a < 4; ++a) {
          const u32x2 v = bp[2 * a];
          xr[4 * a] = bflo(v.x) + bflo(hk[2 * a]); xr[4 * a + 1] = bfhi(v.x) + bfhi(hk[2 * a]);
          xr[4 * a + 2] = bflo(v.y) + bflo(hk[2 * a + 1]); xr[4 * a + 3] = bfhi(v.y) + bfhi(hk[2 * a + 1]);
        }
#pragma unroll
        for (int e = 0; e < 16; ++e) { acc[0][e] = kh ? 0.f : xr[e]; acc[1][e] = kh ? xr[e] : 0.f; }
        TIE(acc[0]); TIE(acc[1]);
      }
      if (w == 0) poll_flag(flags + 64 * (48 + pi), 15u, tmo);
      BAR();
      u32x4 af[16];
      Frag bf[4][2];
      unsigned tb[4];
#pragma unroll
      for (int q = 0; q < 4; ++q) tb[q] = (unsigned)(kh * 81920 + ((q + rb) & 3) * 20480) + tr;
      const char* hb = ws + WS_HID + (size_t)(((rbase >> 5) + mh) * 86 + 43 * kh) * 1024;
      auto lda = [&](auto KS) {
        constexpr int ks = decltype(KS)::v;
        if constexpr (ks < 40) af[ks & 15] = gload16_sc1<(ks % 10 & 3) * 1024>(hb + ((ks / 10 + rb) & 3) * 10240 + (ks % 10 >> 2) * 4096, lane * 16);
        else af[ks & 15] = gload16_sc1<(ks - 40) * 1024>(hb + 40960, lane * 16);
      };
      auto ldb = [&](auto KS) {
        constexpr int ks = decltype(KS)::v, q = ks < 40 ? ks / 10 : 0, sl = ks < 40 ? ks % 10 : ks - 40;
        trf<sl * 2048>(bf[ks & 3][0], tb[q]); trf<sl * 2048 + 1024>(bf[ks & 3][1], tb[q]);
      };
      sfor<0, 16>(lda);
      ldb(IC<0>{}); ldb(IC<1>{}); ldb(IC<2>{}); ldb(IC<3>{});
      BAR();
      sfor<0, 43>([&](auto KS) {
        constexpr int ks = decltype(KS)::v;
        if constexpr (ks == 36) BAR();
        if constexpr (ks < 41) WAITL2(8, bf[ks & 3][0], bf[ks & 3][1]);
        else if constexpr (ks == 41) WAITL2(4, bf[ks & 3][0], bf[ks & 3][1]);
        else WAITL2(0, bf[ks & 3][0], bf[ks & 3][1]);
        asm volatile("s_waitcnt vmcnt(%1)" : "+v"(af[ks & 15]) : "i"(ks <= 27 ? 15 : 42 - ks));
        acc[0] = mfma(cat(bf[ks & 3][0]), af[ks & 15], acc[0]); acc[1] = mfma(cat(bf[ks & 3][1]), af[ks & 15], acc[1]);
        PIN2(acc[0], acc[1]);
        if constexpr (ks + 16 < 43) lda(IC<ks + 16>{});
        if constexpr (ks + 4 < 43) ldb(IC<ks + 4>{});
        SB();
      });
#pragma unroll
      for (int a = 0; a < 4; ++a) {
        f32x4 v;
#pragma unroll
        for (int r = 0; r < 4; ++r) v[r] = kh ? acc[0][4 * a + r] : acc[1][4 * a + r];
        lds_st<f32x4>((unsigned)(kh * 81920 + rb * 20480 + 8192 + mh * 4096 + a * 1024 + lane * 16), v);
      }
      BAR();
      unsigned ok[8];
#pragma unroll
      for (int a = 0; a < 4; ++a) {
        const f32x4 v = lds_ld<f32x4>((unsigned)((kh ^ 1) * 81920 + rb * 20480 + 8192 + mh * 4096 + a * 1024 + lane * 16));
        float o4[4];
#pragma unroll
        for (int r = 0; r < 4; ++r) o4[r] = (kh ? acc[1][4 * a + r] : acc[0][4 * a + r]) + v[r];
        ok[2 * a] = pk_bf16(o4[0], o4[1]); ok[2 * a + 1] = pk_bf16(o4[2], o4[3]);
      }
      GAS u32x4* op = (GAS u32x4*)((char*)out + (size_t)(rbase + 32 * mh + r32) * 1024 + (cbase + 32 * kh + 8 * h5) * 2);
      op[0] = widen(ok[0], ok[1], ok[2], ok[3]);
      op[2] = widen(ok[4], ok[5], ok[6], ok[7]);
    }
  } else {
    const char* w2b = (const char*)w2 + cbase * 2;
    auto w2_slot = [](int q) { const int id = 4 * q; if (id < 60) return 48 + (id >> 1); const int sl = (id - 60) >> 1; return sl < 48 ? sl : sl + 30; };
    u32x4 r2[43];
    const char* w2h = w2b + (hw >> 1) * 16384 + (hw & 1) * 64;
    const char* w2l = w2b + (hw >> 1) * 43 * 16384 + (hw & 1) * 64;
    const unsigned l2h = (unsigned)((hw >> 1) * 2048 + (hw & 1) * 1024 + lane * 16), l2l = (unsigned)((hw >> 1) * 81920 + rb * 20480 + (hw & 1) * 1024 + lane * 16);
    auto ld_w2 = [&](int q) {
      if (q < 40) { const int sl = w2_slot(q); r2[q] = gload16(w2h + (size_t)(sl < 40 ? sl : sl + 3) * 16384, wsq); }
      else r2[q] = gload16(w2l + (size_t)q * 16384, wsq);
    };
    auto st_w2 = [&](int q) { lds_st<u32x4>(q < 40 ? l2h + (unsigned)w2_slot(q) * 2048 : l2l + (unsigned)(q - 40) * 2048, r2[q]); };
    float c0 = 0.f, c1 = 0.f;
    auto colsum8 = [&](unsigned base) {
      u32x4 cf[8];
#pragma unroll
      for (int s = 0; s < 8; ++s) cf[s] = trfrag(base + s * 1024, 256);
#pragma unroll
      for (int s = 0; s < 8; ++s) { c0 = dot2acc(cf[s][0], ONES2, c0); c1 = dot2acc(cf[s][1], ONES2, c1); c0 = dot2acc(cf[s][2], ONES2, c0); c1 = dot2acc(cf[s][3], ONES2, c1); }
      SB();
    };
    auto stage_w1 = [&](int q) { const int id = 4 * q + hw; glds16((q < 8 ? w1b : w1c) + (size_t)(id & 31) * 16 * 2752 + (q >> 3) * 64, w1v, (unsigned)id * 1024); };
    if (w == 7 && lane < 20) lds_st<unsigned>(SC_ZERO + (unsigned)lane * 4, 0u);
    BAR();
    const int bi = 96 * j + 32 * hw + r32;
    float csb = bflo((unsigned)((const GAS u16*)b1)[bi < 1376 ? bi : 1375]);
    TIE(csb);
    {
      const char* wob = (const char*)wo + cbase * 2;
#pragma unroll
      for (int q = 0; q < 16; ++q) { const int id = 4 * q + hw; glds16(wob + (size_t)(id >> 1) * 16384 + (id & 1) * 64, wsq, RB + (unsigned)id * 1024); }
    }
    lds_wait(SC_CC, 4u);
#pragma unroll
    for (int q = 0; q < 11; ++q) stage_w1(q);
    WAITV(11);
    lds_add(SC_HQ, lane);
    lds_wait(SC_CC, 8u);
#pragma unroll
    for (int q = 11; q < 22; ++q) stage_w1(q);
#pragma unroll
    for (int q = 0; q < 16; ++q) ld_w2(q);
    BAR();
    stage_w1(22); stage_w1(23);
    WAITV(0);
    BAR();
    if (hw < 3) {
      const unsigned base = (unsigned)hw * 32768 + tr;
      colsum8(base); colsum8(base + 8192); colsum8(base + 16384); colsum8(base + 24576);
      const float c = half_sum(c0 + c1);
      lds_st<float>(SC_C + (unsigned)(h5 * 128 + hw * 32 + r32) * 4, h5 ? csb : c);
    }
#pragma unroll
    for (int q = 16; q < 32; ++q) ld_w2(q);
#pragma unroll
    for (int q = 0; q < 15; ++q) { TIE(r2[q]); st_w2(q); }
    BAR();
    __builtin_amdgcn_s_sleep(64);
#pragma unroll
    for (int q = 32; q < 43; ++q) ld_w2(q);
    WAITV(0);
#pragma unroll
    for (int q = 15; q < 43; ++q) TIE(r2[q]);
    BAR();
#pragma unroll
    for (int q = 15; q < 40; ++q) st_w2(q);
    BAR();
    BAR();
    st_w2(40); st_w2(41); st_w2(42);
    BAR();
    BAR();
  }
}

extern "C" void kernel_launch(void* const* d_in, const int* in_sizes, int n_in, void* d_out, int out_size, void* d_ws, size_t ws_size,
                              hipStream_t stream) {
  (void)in_sizes; (void)n_in; (void)out_size; (void)ws_size;
  (void)hipFuncSetAttribute((const void*)attn_front, hipFuncAttributeMaxDynamicSharedMemorySize, LDS_BYTES);
  (void)hipFuncSetAttribute((const void*)block_tail, hipFuncAttributeMaxDynamicSharedMemorySize, LDS_BYTES);
  attn_front<<<256, 512, LDS_BYTES, stream>>>((const u16*)d_in[0], (const u16*)d_in[1], (const u16*)d_in[2], (const u16*)d_in[3], (const u16*)d_in[5],
                                              (const u16*)d_in[7], (char*)d_ws);
  block_tail<<<256, 512, LDS_BYTES, stream>>>((const u16*)d_in[0], (const u16*)d_in[3], (const u16*)d_in[5], (const u16*)d_in[6],
                                              (const u16*)d_in[7], (const u16*)d_in[8], (u16*)d_out, (char*)d_ws);
}
```
